```python
import torch

def _softmax(x, axis=-1):
    return torch.softmax(x, dim=axis)

def _gelu(x):
    return torch.nn.functional.gelu(x, approximate='tanh')

def _silu(x):
    return torch.nn.functional.silu(x)

def _sigmoid(x):
    return torch.sigmoid(x)

def _layernorm(x, axis=-1, eps=1e-05):
    m = x.mean(dim=axis, keepdim=True)
    v = x.var(dim=axis, keepdim=True, unbiased=False)
    return (x - m) / torch.sqrt(v + eps)

def _scatter_add(tgt, idx, src, axis=0):
    return tgt.clone().index_add_(axis, idx, src)

def _segment_sum(src, seg, n):
    out = torch.zeros((n,) + src.shape[1:], dtype=src.dtype)
    return out.index_add_(0, seg, src)

def _pad(x, pw):
    flat = tuple((v for pair in reversed(pw) for v in pair))
    return torch.nn.functional.pad(x, flat)
kernel_name = 'adaLN_cond__ssm_conv_h3__glu_gated_glu__prompt'

def setup_inputs():

    _w = lambda a: (lambda t: t.to(torch.bfloat16) if t.is_floating_point() else t)(torch.as_tensor(a))

    def _A_setup():
        B, S, D = (1, 2048, 576)
        ks = 4
        return {'q': _w((lambda *s: torch.randn(*s, dtype=torch.bfloat16) * 0.05)(B, S, D)), 'k': _w((lambda *s: torch.randn(*s, dtype=torch.bfloat16) * 0.8)(B, S, D)), 'v': _w((lambda *s: torch.randn(*s, dtype=torch.bfloat16) * 0.8)(B, S, D)), 'shift_k': _w((lambda *s: torch.randn(*s, dtype=torch.bfloat16) * 0.8)(D, ks) * 0.5), 'ssm_k': _w((lambda *s: torch.randn(*s, dtype=torch.bfloat16) * 0.8)(D, S) * (1.0 / S ** 0.5)), 'ssm_D': _w((lambda *s: torch.randn(*s, dtype=torch.bfloat16) * 0.8)(D))}
    _A_in = _A_setup()

    def _B_setup():
        B, S, D, F = (1, 2048, 576, 4608)
        return {'x': _w((lambda *s: torch.randn(*s, dtype=torch.bfloat16) * 0.05)(B, S, D)), 'w_gate': _w((lambda *s: torch.randn(*s, dtype=torch.bfloat16) * 0.05)(D, F)), 'w_up': _w((lambda *s: torch.randn(*s, dtype=torch.bfloat16) * 0.05)(D, F)), 'w_down': _w((lambda *s: torch.randn(*s, dtype=torch.bfloat16) * 0.05)(F, D))}
    _B_in = _B_setup()
    return {'A_q': _A_in['q'], 'A_k': _A_in['k'], 'A_v': _A_in['v'], 'A_shift_k': _A_in['shift_k'], 'A_ssm_k': _A_in['ssm_k'], 'A_ssm_D': _A_in['ssm_D'], 'B_x': _B_in['x'], 'B_w_gate': _B_in['w_gate'], 'B_w_up': _B_in['w_up'], 'B_w_down': _B_in['w_down'], 'c': _w((lambda *s: torch.randn(*s, dtype=torch.bfloat16) * 0.1)(1, 576) * 8.0), 'w_mod': _w((lambda *s: torch.randn(*s, dtype=torch.bfloat16) * 0.1)(576, 6 * 576) * 576 ** (-0.5) * 8.0)}

def reference(A_q, A_k, A_v, A_shift_k, A_ssm_k, A_ssm_D, B_x, B_w_gate, B_w_up, B_w_down, c, w_mod):

    def _A_ref(q, k, v, shift_k, ssm_k, ssm_D):
        B, S, D = q.shape
        ks = shift_k.shape[-1]
        k_pad = _pad(k, ((0, 0), (ks - 1, 0), (0, 0)))
        k_shift = torch.zeros_like(k)
        for t in range(ks):
            k_shift = k_shift + k_pad[:, t:t + S, :] * shift_k[:, ks - 1 - t][None, None, :]
        kv = k_shift * v
        L = 2 * S
        kv_pad = _pad(kv, ((0, 0), (0, S), (0, 0)))
        kf_pad = _pad(ssm_k, ((0, 0), (0, S)))
        kv_f = (lambda x, axes=(-2, -1): torch.fft.rfft2(x.float(), dim=axes))(kv_pad[:, None, :, :], axes=(1, 2))
        kf_f = (lambda x, axes=(-2, -1): torch.fft.rfft2(x.float(), dim=axes))(kf_pad[:, None, :], axes=(1, 2))
        kf_f = (lambda x, *d: x.permute(*d))(kf_f, 1, 2, 0)
        y_f = kv_f * kf_f[None, :, :, :]
        y = (lambda x, s, axes=(-2, -1): torch.fft.irfft2(x, s=s, dim=axes).to(torch.bfloat16))(y_f, (1, L), axes=(1, 2))[:, 0, :S, :]
        y = y + kv * ssm_D[None, None, :]
        return q * y

    def _B_ref(x, w_gate, w_up, w_down):
        g = torch.einsum('bsd,df->bsf', x, w_gate)
        u = torch.einsum('bsd,df->bsf', x, w_up)
        h = _sigmoid(g) * u
        return torch.einsum('bsf,fd->bsd', h, w_down)
    x = A_q
    mod = (_silu(c) @ w_mod).reshape(c.shape[0], 6, 1, -1)
    sa, ca, ga, sb, cb, gb = (mod[:, 0].clone(), mod[:, 1].clone(), mod[:, 2].clone(), mod[:, 3].clone(), mod[:, 4].clone(), mod[:, 5].clone())
    h = _layernorm(x, axis=-1) * (1 + ca) + sa
    x = x + ga * _A_ref(h, A_k, A_v, A_shift_k, A_ssm_k, A_ssm_D)
    h = _layernorm(x, axis=-1) * (1 + cb) + sb
    x = x + gb * _B_ref(h, B_w_gate, B_w_up, B_w_down)
    return x
```

```cpp
#include <hip/hip_runtime.h>
#include <hip/hip_bf16.h>
#include <stdint.h>

typedef unsigned short u16;
static __device__ __forceinline__ float bf2f(u16 v) { return __uint_as_float(((unsigned)v) << 16); }
static __device__ __forceinline__ u16 f2bf(float f) { __hip_bfloat16 b = __float2bfloat16(f); return *(u16*)&b; }
static __device__ __forceinline__ float rb(float f) { return bf2f(f2bf(f)); }

constexpr int S = 2048, D = 576, F = 4608;

__global__ void k_mod(const u16* c, const u16* w, u16* mod) {
  int n = blockIdx.x * blockDim.x + threadIdx.x;
  if (n >= 6 * D) return;
  float acc = 0;
  for (int k = 0; k < D; ++k) {
    float x = bf2f(c[k]);
    float s = rb(x / (1.f + __expf(-x)));
    acc += s * bf2f(w[(size_t)k * 6 * D + n]);
  }
  mod[n] = f2bf(acc);
}
__global__ void k_kv(const u16* k, const u16* v, const u16* sk, u16* kv) {
  int i = blockIdx.x * blockDim.x + threadIdx.x;
  if (i >= S * D) return;
  int s = i / D, d = i % D;
  float acc = 0;
  for (int t = 0; t < 4; ++t) {
    int ss = s + t - 3;
    float kk = ss >= 0 ? bf2f(k[ss * D + d]) : 0.f;
    acc = rb(acc + rb(kk * bf2f(sk[d * 4 + 3 - t])));
  }
  kv[i] = f2bf(acc * bf2f(v[i]));
}
__global__ void k_conv(const u16* kv, const u16* f, const u16* Dv, u16* y) {
  int i = blockIdx.x * blockDim.x + threadIdx.x;
  if (i >= S * D) return;
  int s = i / D, d = i % D;
  float acc = 0;
  for (int j = 0; j <= s; ++j) acc += bf2f(kv[j * D + d]) * bf2f(f[d * S + s - j]);
  float yy = rb(acc);
  y[i] = f2bf(yy + rb(bf2f(kv[i]) * bf2f(Dv[d])));
}
__device__ float wsum(float v) { for (int o = 32; o; o >>= 1) v += __shfl_xor(v, o); return v; }
__global__ void k_ln(const u16* q, const u16* y, const u16* mod, u16* x1, u16* h2) {
  int s = blockIdx.x; int l = threadIdx.x;
  float x[9];
  float sum = 0;
  for (int i = 0; i < 9; ++i) { x[i] = bf2f(q[s * D + l + 64 * i]); sum += x[i]; }
  float mean = wsum(sum) / D;
  float vs = 0;
  for (int i = 0; i < 9; ++i) { float d = x[i] - mean; vs += d * d; }
  float var = rb(wsum(vs) / D);
  float m = rb(mean);
  float sd = rb(sqrtf(rb(var + 1e-5f)));
  sum = 0;
  for (int i = 0; i < 9; ++i) {
    int d = l + 64 * i;
    float ln = rb(rb(x[i] - m) / sd);
    float h = rb(rb(ln * rb(1.f + bf2f(mod[D + d]))) + bf2f(mod[d]));
    float a = rb(h * bf2f(y[s * D + d]));
    x[i] = rb(x[i] + rb(bf2f(mod[2 * D + d]) * a));
    x1[s * D + d] = f2bf(x[i]);
    sum += x[i];
  }
  mean = wsum(sum) / D;
  vs = 0;
  for (int i = 0; i < 9; ++i) { float d = x[i] - mean; vs += d * d; }
  var = rb(wsum(vs) / D);
  m = rb(mean);
  sd = rb(sqrtf(rb(var + 1e-5f)));
  for (int i = 0; i < 9; ++i) {
    int d = l + 64 * i;
    float ln = rb(rb(x[i] - m) / sd);
    float h = rb(rb(ln * rb(1.f + bf2f(mod[4 * D + d]))) + bf2f(mod[3 * D + d]));
    h2[s * D + d] = f2bf(h);
  }
}
__global__ void k_g1(const u16* h, const u16* wg, const u16* wu, u16* act) {
  int f = blockIdx.x * blockDim.x + threadIdx.x; int s = blockIdx.y;
  float g = 0, u = 0;
  for (int k = 0; k < D; ++k) { float hh = bf2f(h[s * D + k]); g += hh * bf2f(wg[(size_t)k * F + f]); u += hh * bf2f(wu[(size_t)k * F + f]); }
  g = rb(g); u = rb(u);
  float sg = rb(1.f / (1.f + __expf(-g)));
  act[(size_t)s * F + f] = f2bf(sg * u);
}
__global__ void k_g2(const u16* act, const u16* wd, const u16* x1, const u16* mod, u16* out) {
  int n = blockIdx.x * blockDim.x + threadIdx.x; int s = blockIdx.y;
  float a = 0;
  for (int k = 0; k < F; ++k) a += bf2f(act[(size_t)s * F + k]) * bf2f(wd[(size_t)k * D + n]);
  a = rb(a);
  out[s * D + n] = f2bf(bf2f(x1[s * D + n]) + rb(bf2f(mod[5 * D + n]) * a));
}

extern "C" void kernel_launch(void* const* d_in, const int* in_sizes, int n_in,
                              void* d_out, int out_size, void* d_ws, size_t ws_size,
                              hipStream_t stream) {
  const u16* q = (const u16*)d_in[0]; const u16* k = (const u16*)d_in[1]; const u16* v = (const u16*)d_in[2];
  const u16* sk = (const u16*)d_in[3]; const u16* f = (const u16*)d_in[4]; const u16* Dv = (const u16*)d_in[5];
  const u16* wg = (const u16*)d_in[7]; const u16* wu = (const u16*)d_in[8]; const u16* wd = (const u16*)d_in[9];
  const u16* c = (const u16*)d_in[10]; const u16* wm = (const u16*)d_in[11];
  char* ws = (char*)d_ws;
  u16* mod = (u16*)ws; u16* kv = (u16*)(ws + 65536); u16* y = kv + S * D; u16* x1 = y + S * D; u16* h2 = x1 + S * D; u16* act = h2 + S * D;
  k_mod<<<(6 * D + 255) / 256, 256, 0, stream>>>(c, wm, mod);
  k_kv<<<S * D / 256, 256, 0, stream>>>(k, v, sk, kv);
  k_conv<<<S * D / 256, 256, 0, stream>>>(kv, f, Dv, y);
  k_ln<<<S, 64, 0, stream>>>(q, y, mod, x1, h2);
  k_g1<<<dim3(F / 256, S), 256, 0, stream>>>(h2, wg, wu, act);
  k_g2<<<dim3(D / 64, S), 64, 0, stream>>>(act, wd, x1, mod, (u16*)d_out);
}
```

```cpp
#include <hip/hip_runtime.h>
#include <hip/hip_bf16.h>
#include <stdint.h>
#include <type_traits>

typedef unsigned short u16;
typedef short s16x4 __attribute__((ext_vector_type(4)));
typedef short s16x8 __attribute__((ext_vector_type(8)));
typedef __bf16 bf16x8 __attribute__((ext_vector_type(8)));
typedef float f32x4 __attribute__((ext_vector_type(4)));
typedef float f32x2 __attribute__((ext_vector_type(2)));
typedef unsigned u32x4 __attribute__((ext_vector_type(4)));
typedef unsigned u32x2 __attribute__((ext_vector_type(2)));
#define LAS __attribute__((address_space(3)))

constexpr int S = 2048, D = 576, F = 4608;

constexpr size_t WS_EPOCH = 0;
constexpr size_t WS_FA = 4096;
constexpr size_t WS_FB = 8192;
constexpr size_t WS_FP = WS_FB + 16384;
constexpr size_t WS_FL = WS_FP + 16384;
constexpr size_t WS_FG = WS_FL + 4096;
constexpr size_t WS_FX = WS_FG + 4096;
constexpr size_t WS_MOD = 65536;
constexpr size_t WS_KVT = 131072;
constexpr size_t SZ_SD = (size_t)S * D * 2;
constexpr size_t WS_YT = WS_KVT + SZ_SD;
constexpr size_t WS_X1 = WS_YT + SZ_SD;
constexpr size_t WS_H2 = WS_X1 + SZ_SD;
constexpr size_t WS_ACT = WS_H2 + SZ_SD;
constexpr size_t WS_WDT = WS_ACT + (size_t)S * F * 2;
constexpr size_t WS_PART = WS_WDT + (size_t)F * D * 2;

typedef __bf16 bf16x2 __attribute__((ext_vector_type(2)));
__device__ __forceinline__ unsigned pk2(float lo, float hi) { const f32x2 v = {lo, hi}; return __builtin_bit_cast(unsigned, __builtin_convertvector(v, bf16x2)); }
__device__ __forceinline__ float lo_f(unsigned v) { return __uint_as_float(v << 16); }
__device__ __forceinline__ float hi_f(unsigned v) { return __uint_as_float(v & 0xffff0000u); }
__device__ __forceinline__ f32x2 up2(unsigned v) { return (f32x2){lo_f(v), hi_f(v)}; }
__device__ __forceinline__ f32x2 rb2(f32x2 v) { return up2(pk2(v.x, v.y)); }
__device__ __forceinline__ float rbf(float f) { return lo_f(pk2(f, f)); }
__device__ __forceinline__ float bf2f(u16 v) { return __uint_as_float(((unsigned)v) << 16); }

__device__ __forceinline__ u32x4 make_rsrc(const void* p, unsigned bytes) {
  unsigned long long a = (unsigned long long)p;
  u32x4 r; r.x = (unsigned)a; r.y = (unsigned)(a >> 32) & 0xffffu; r.z = bytes; r.w = 0x00020000u; return r;
}
#define SETM0(ldsaddr) asm volatile("s_mov_b32 m0, %0" :: "s"(ldsaddr) : "memory")
#define DMA(voff, rsrc, soff, IMM) asm volatile("buffer_load_dwordx4 %0, %1, %2 offen offset:" #IMM " lds" :: "v"(voff), "s"(rsrc), "s"(soff) : "memory")
#define DMAC(voff, rsrc, soff, IMM) asm volatile("buffer_load_dwordx4 %0, %1, %2 offen offset:" #IMM " sc1 lds" :: "v"(voff), "s"(rsrc), "s"(soff) : "memory")
#define NOP0() asm volatile("s_nop 0")
#define MFMA_ASM(acc, a, b) asm volatile("v_mfma_f32_16x16x32_bf16 %0, %1, %2, %0" : "+v"(acc) : "v"(a), "v"(b))
#define WAIT_V(n) asm volatile("s_waitcnt vmcnt(" #n ")" ::: "memory")
#define WAIT_L0() __builtin_amdgcn_s_waitcnt(0xC07F)
#define SB() __builtin_amdgcn_sched_barrier(0)
#define BAR() do { SB(); asm volatile("s_barrier" ::: "memory"); SB(); } while (0)
__device__ __forceinline__ void st16_dev(void* p, u32x4 v) { asm volatile("global_store_dwordx4 %0, %1, off sc1\n\ts_nop 1" :: "v"(p), "v"(v) : "memory"); }
__device__ __forceinline__ void st4_dev(void* p, unsigned v) { asm volatile("global_store_dword %0, %1, off sc1" :: "v"(p), "v"(v) : "memory"); }

__device__ __forceinline__ bf16x8 lds_b128(unsigned a) { return __builtin_bit_cast(bf16x8, *(LAS const s16x8*)(size_t)a); }
__device__ __forceinline__ s16x4 lds_tr(unsigned a) { return __builtin_amdgcn_ds_read_tr16_b64_v4i16((LAS s16x4*)(size_t)a); }
__device__ __forceinline__ bf16x8 cat8(s16x4 a, s16x4 b) { return __builtin_bit_cast(bf16x8, __builtin_shufflevector(a, b, 0, 1, 2, 3, 4, 5, 6, 7)); }
__device__ __forceinline__ int pi_row(int k) { return k ^ (((k >> 3) & 1) << 2); }
__device__ __forceinline__ int opaque_tid() { int t = threadIdx.x; asm volatile("" : "+v"(t)); return t; }

__device__ __forceinline__ void publish(char* flag0, int nrep, int repstride, unsigned epoch) {
  WAIT_V(0); WAIT_L0();
  BAR();
  if (threadIdx.x < (unsigned)nrep) st4_dev(flag0 + (size_t)threadIdx.x * repstride, epoch);
}
template <int COUNT>
__device__ __forceinline__ void poll_flags(const char* p, unsigned epoch, int lane, int skip = -1) {
  constexpr int NL = (COUNT + 63) / 64;
  const char* a[NL];
#pragma unroll
  for (int b = 0; b < NL; ++b) { int i = b * 64 + lane; i = i < COUNT ? i : COUNT - 1; i = i == skip ? (skip == 0 ? 1 : 0) : i; a[b] = p + i * 4; }
  for (unsigned spin = 0; spin < (1u << 16); ++spin) {
    unsigned v[NL];
#pragma unroll
    for (int b = 0; b < NL; ++b) asm volatile("global_load_dword %0, %1, off sc1" : "=v"(v[b]) : "v"(a[b]) : "memory");
    bool ok = true;
#pragma unroll
    for (int b = 0; b < NL; ++b) { asm volatile("s_waitcnt vmcnt(0)" : "+v"(v[b]) :: "memory"); ok = ok && v[b] == epoch; }
    if (__all(ok)) break;
    __builtin_amdgcn_s_sleep(2);
  }
}
template <int COUNT>
__device__ __forceinline__ void wait_flags(const char* p, unsigned epoch, int wid, int lane) {
  if (wid == 0) poll_flags<COUNT>(p, epoch, lane);
  BAR();
}

__device__ __forceinline__ void phase_kv(LAS char* sm, const u16* k, const u16* v, const u16* sk, const u16* fk, char* kvT, int cgp, int rb, int ch0, int wid, int lane) {
  const int t = opaque_tid();
  const int o = t >> 5, cp = t & 31;
  const int s0 = rb * 128 + o * 8, d0 = cgp * 64 + 2 * cp;
  unsigned kk[11], vv[8];
  u32x4 w4;
  {
    const u32x4 rK = make_rsrc(k, S * D * 2), rV = make_rsrc(v, S * D * 2), rS = make_rsrc(sk, D * 8);
    const unsigned off = (unsigned)((s0 * D + d0) * 2);
#pragma unroll
    for (int i = 0; i < 11; ++i) asm volatile("buffer_load_dword %0, %1, %2, 0 offen" : "=v"(kk[i]) : "v"(off + (unsigned)((i - 3) * D * 2)), "s"(rK) : "memory");
#pragma unroll
    for (int i = 0; i < 8; ++i) asm volatile("buffer_load_dword %0, %1, %2, 0 offen" : "=v"(vv[i]) : "v"(off + (unsigned)(i * D * 2)), "s"(rV) : "memory");
    asm volatile("buffer_load_dwordx4 %0, %1, %2, 0 offen" : "=v"(w4) : "v"((unsigned)(d0 * 8)), "s"(rS) : "memory");
  }
  if (wid < 2) {
    unsigned dummy;
    asm volatile("global_load_dword %0, %1, off" : "=v"(dummy) : "v"((const char*)fk + (size_t)ch0 * 4096 + (wid * 64 + lane) * 128) : "memory");
    WAIT_V(0);
    asm volatile("" :: "v"(dummy));
  } else WAIT_V(0);
#pragma unroll
  for (int i = 0; i < 11; ++i) asm volatile("" : "+v"(kk[i]));
#pragma unroll
  for (int i = 0; i < 8; ++i) asm volatile("" : "+v"(vv[i]));
  asm volatile("" : "+v"(w4));
  SB();
  const f32x2 wv[4] = {{lo_f(w4.x), lo_f(w4.z)}, {hi_f(w4.x), hi_f(w4.z)}, {lo_f(w4.y), lo_f(w4.w)}, {hi_f(w4.y), hi_f(w4.w)}};
  f32x2 kf[11];
#pragma unroll
  for (int i = 0; i < 11; ++i) kf[i] = up2(kk[i]);
  float x[2][8];
#pragma unroll
  for (int i = 0; i < 8; ++i) {
    f32x2 a = rb2(rb2(kf[i] * wv[3]) + rb2(kf[i + 1] * wv[2]));
    a = rb2(a + rb2(kf[i + 2] * wv[1]));
    a = rb2(a + rb2(kf[i + 3] * wv[0]));
    a = a * up2(vv[i]);
    x[0][i] = a.x; x[1][i] = a.y;
  }
  asm volatile("" :: "v"(x[0][0]), "v"(x[1][7]));

#pragma unroll
  for (int c = 0; c < 2; ++c) {
    u32x4 p; p.x = pk2(x[c][7], x[c][6]); p.y = pk2(x[c][5], x[c][4]); p.z = pk2(x[c][3], x[c][2]); p.w = pk2(x[c][1], x[c][0]);
    *(LAS u32x4*)(sm + (2 * cp + c) * 256 + ((o ^ (cp & 7)) * 16)) = p;
  }
  WAIT_L0(); BAR();
#pragma unroll
  for (int it = 0; it < 2; ++it) {
    const int idx = it * 512 + t, ch = idx >> 4, oo = idx & 15;
    const u32x4 p = *(LAS const u32x4*)(sm + ch * 256 + ((oo ^ ((ch >> 1) & 7)) * 16));
    st16_dev(kvT + (size_t)(cgp * 64 + ch) * 4096 + (rb * 16 + oo) * 16, p);
  }
}

constexpr int CV_CH = 4 * 4160 + 288 * 16;
constexpr int CV_XR = 4 * 4160;
constexpr int CV_STAGE = 4 * CV_CH;
__device__ __forceinline__ void conv_load_f(LAS char* lds, const u16* fk, int ch0, int wid, int lane) {
  if (wid < 4) return;
  const int c = wid & 3, ch = ch0 + c;
  const unsigned base = (unsigned)(size_t)lds + c * CV_CH;
  const u32x4 rF = make_rsrc(fk, D * S * 2);
  const unsigned zero = 0;
#pragma unroll
  for (int r = 0; r < 4; ++r) {
    const unsigned vf = (unsigned)(ch * 4096 + (r + 17) * 2 + lane * 16);
    SETM0(base + r * 4160 + 64); NOP0();
    DMA(vf, rF, zero, 0); DMA(vf, rF, zero, 1024); DMA(vf, rF, zero, 2048); DMA(vf, rF, zero, 3072);
  }
}
__device__ __forceinline__ void conv_front(LAS char* lds, const u16* fk, int ch0, int wid, int lane) {
  const int c = wid & 3, h = wid >> 2, ch = ch0 + c;
  if (h == 0) {
    const int r = lane >> 4, e = lane & 15, i0 = 2 * e + r - 15;
    const unsigned a = i0 >= 0 ? fk[ch * S + i0] : 0u, b = i0 + 1 >= 0 ? fk[ch * S + i0 + 1] : 0u;
    *(LAS unsigned*)(lds + c * CV_CH + r * 4160 + e * 4) = a | (b << 16);
  } else if (lane < 32) *(LAS u32x4*)(lds + c * CV_CH + CV_XR + lane * 16) = (u32x4){0u, 0u, 0u, 0u};
}
template <class Hook>
__device__ __forceinline__ void phase_conv(LAS char* lds, const char* kvT, const u16* Dv, char* yT, int ch0, int wid, int lane, Hook hook) {
  const int c = wid & 3, h = wid >> 2, ch = ch0 + c;
  const unsigned base = (unsigned)(size_t)lds + c * CV_CH;
  {
    const u32x4 rX = make_rsrc(kvT, D * S * 2);
    const unsigned zero = 0;
    const unsigned vx = (unsigned)(ch * 4096 + h * 2048 + lane * 16);
    SETM0(base + CV_XR + 512 + h * 2048); NOP0();
    DMAC(vx, rX, zero, 0); DMAC(vx, rX, zero, 1024);
  }
  const float Dc = bf2f(Dv[ch]);
  WAIT_V(0); WAIT_L0(); BAR();

  hook();
  const int n = lane & 15, g = lane >> 4;
  unsigned aA0 = base + (n & 3) * 4160 + 2 * (8 * g + 4 * (n >> 2)) + 256 * h;
  unsigned aA1 = aA0 + 8;
  unsigned aB = base + CV_XR + 16 * (32 + 2 * n + 1 - g) - 256 * h;
  asm volatile("" : "+v"(aA0), "+v"(aA1), "+v"(aB));
  f32x4 acc[8];
#pragma unroll
  for (int t = 0; t < 8; ++t) { acc[t] = (f32x4){0.f, 0.f, 0.f, 0.f}; asm volatile("" : "+v"(acc[t])); }
  bf16x8 B[2][8]; s16x4 Alo[2][8], Ahi[2][8];
  auto rdB = [&](int bu, int rr, int t) { B[bu][t] = lds_b128(aB - 64 * rr + 512 * t); };
  auto rdA = [&](int bu, int rr, int q) { Alo[bu][q] = *(volatile LAS const s16x4*)(size_t)(aA0 + 64 * rr + 512 * q); Ahi[bu][q] = *(volatile LAS const s16x4*)(size_t)(aA1 + 64 * rr + 512 * q); };
#pragma unroll
  for (int t = 0; t < 8; ++t) { rdB(0, 0, t); rdA(0, 0, t); }
#pragma unroll
  for (int rr = 0; rr < 4; ++rr) {
    const int cur = rr & 1, nxt = cur ^ 1;
    int k = 0;
#pragma unroll
    for (int q = 0; q < 8; ++q) {
      const bf16x8 A = cat8(Alo[cur][q], Ahi[cur][q]);
#pragma unroll
      for (int t = q; t < 8; ++t) {
        MFMA_ASM(acc[t], A, B[cur][t - q]);
        SB();
        if (rr < 3) { if (k < 8) rdB(nxt, rr + 1, k); else if (k < 16) rdA(nxt, rr + 1, k - 8); }
        ++k;
        SB();
      }
    }
  }
  asm volatile("s_nop 7\n\ts_nop 7" ::: "memory");
  SB();

  BAR();
  LAS char* xb = lds + c * CV_CH;
#pragma unroll
  for (int t = 0; t < 4; ++t) *(LAS f32x4*)(xb + (h * 4 + t) * 1024 + lane * 16) = h ? acc[t] : acc[4 + t];
  WAIT_L0(); BAR();
  unsigned pk[4][2];
#pragma unroll
  for (int t = 0; t < 4; ++t) {
    const f32x4 o = *(LAS const f32x4*)(xb + ((h ^ 1) * 4 + t) * 1024 + lane * 16);
    const f32x4 a = (h ? acc[4 + t] : acc[t]) + o;
    const int tt = 4 * h + t;
    const int u = 32 * tt + 2 * n + (g >> 1);
    const u32x2 xx = *(LAS const u32x2*)(lds + c * CV_CH + CV_XR + 16 * (32 + u) + ((g & 1) ? 0 : 8));
    const float x0 = hi_f(xx.y), x1 = lo_f(xx.y), x2 = hi_f(xx.x), x3 = lo_f(xx.x);
    const unsigned c01 = pk2(a[0], a[1]), c23 = pk2(a[2], a[3]);
    const unsigned m01 = pk2(x0 * Dc, x1 * Dc), m23 = pk2(x2 * Dc, x3 * Dc);
    pk[t][0] = pk2(lo_f(c01) + lo_f(m01), hi_f(c01) + hi_f(m01));
    pk[t][1] = pk2(lo_f(c23) + lo_f(m23), hi_f(c23) + hi_f(m23));
  }
#pragma unroll
  for (int p = 0; p < 2; ++p) {
    auto r0 = __builtin_amdgcn_permlane16_swap(pk[2 * p][0], pk[2 * p + 1][0], false, false);
    auto r1 = __builtin_amdgcn_permlane16_swap(pk[2 * p][1], pk[2 * p + 1][1], false, false);
    u32x4 o; o.x = r0[0]; o.y = r1[0]; o.z = r0[1]; o.w = r1[1];
    const int tt = 4 * h + 2 * p + (g & 1);
    st16_dev(yT + (size_t)ch * 4096 + (256 * tt + 16 * n + 8 * (g >> 1)) * 2, o);
  }
}

__device__ __forceinline__ void phase_mod(LAS char* lds, const u16* c, const u16* wm, char* modf, int j) {
  const int t = opaque_tid(), cc = t & 7, kg = t >> 3;
  u32x4 wv[9]; unsigned cv[9];
#pragma unroll
  for (int i = 0; i < 9; ++i) wv[i] = *(const u32x4*)(wm + (size_t)(kg * 9 + i) * (6 * D) + j * 64 + cc * 8);
#pragma unroll
  for (int i = 0; i < 9; ++i) cv[i] = c[kg * 9 + i];
  float acc[8] = {0, 0, 0, 0, 0, 0, 0, 0};
#pragma unroll
  for (int i = 0; i < 9; ++i) {
    const float x = __uint_as_float(cv[i] << 16);
    const float sl = rbf(x * __builtin_amdgcn_rcpf(1.f + __builtin_amdgcn_exp2f(x * -1.44269504089f)));
    acc[0] = fmaf(sl, lo_f(wv[i].x), acc[0]); acc[1] = fmaf(sl, hi_f(wv[i].x), acc[1]);
    acc[2] = fmaf(sl, lo_f(wv[i].y), acc[2]); acc[3] = fmaf(sl, hi_f(wv[i].y), acc[3]);
    acc[4] = fmaf(sl, lo_f(wv[i].z), acc[4]); acc[5] = fmaf(sl, hi_f(wv[i].z), acc[5]);
    acc[6] = fmaf(sl, lo_f(wv[i].w), acc[6]); acc[7] = fmaf(sl, hi_f(wv[i].w), acc[7]);
  }
  LAS float* red = (LAS float*)lds;
  *(LAS f32x4*)(red + kg * 64 + cc * 8) = (f32x4){acc[0], acc[1], acc[2], acc[3]};
  *(LAS f32x4*)(red + kg * 64 + cc * 8 + 4) = (f32x4){acc[4], acc[5], acc[6], acc[7]};
  WAIT_L0(); BAR();
  {
    const int col = t & 63, part8 = t >> 6;
    float s = 0;
#pragma unroll
    for (int i = 0; i < 8; ++i) s += red[(part8 * 8 + i) * 64 + col];
    WAIT_L0(); BAR();
    red[part8 * 64 + col] = s;
  }
  WAIT_L0(); BAR();
  if (t < 64) {
    float s = 0;
#pragma unroll
    for (int i = 0; i < 8; ++i) s += red[i * 64 + t];
    const int col = j * 64 + t, part = col / D, d = col - part * D;
    float m = rbf(s);
    if (part == 1 || part == 4) m = rbf(1.f + m);
    const unsigned mine = __float_as_uint(m) >> 16;
    const unsigned other = (unsigned)__builtin_amdgcn_update_dpp(0, (int)mine, 0xB1, 0xf, 0xf, true);
    if ((t & 1) == 0) st4_dev(modf + (d >> 1) * 32 + part * 4, mine | (other << 16));
  }
}

__device__ __forceinline__ void phase_pretile(LAS char* lds, const u16* wd, char* wdt, int u, int wid, int lane) {
  const unsigned base = (unsigned)(size_t)lds + wid * 9216;
  const u32x4 rW = make_rsrc(wd, F * D * 2);
  const int ct = u / 144, kap = u - ct * 144;
  unsigned so = __builtin_amdgcn_readfirstlane((unsigned)(kap * 32 * D * 2 + ct * 288));
  asm volatile("s_nop 4" : "+s"(so));
#pragma unroll
  for (int j = 0; j < 9; ++j) {
    const int pos = 64 * j + lane, r = pos / 18, c = pos - r * 18;
    const unsigned vo = (unsigned)(pi_row(r) * (D * 2) + c * 16);
    SETM0(base + j * 1024); NOP0(); DMA(vo, rW, so, 0);
  }
  WAIT_V(0);
  const int g = lane >> 4, q = (lane & 15) >> 2, p = lane & 3;
  const unsigned a0 = base + pi_row(8 * g + q) * 288 + p * 8, a1 = base + pi_row(8 * g + 4 + q) * 288 + p * 8;
#pragma unroll
  for (int j = 0; j < 9; ++j) {
    const bf16x8 f = cat8(lds_tr(a0 + j * 32), lds_tr(a1 + j * 32));
    st16_dev(wdt + ((size_t)u * 9 + j) * 1024 + lane * 16, __builtin_bit_cast(u32x4, f));
  }
}

template <int CTRL> __device__ __forceinline__ float dpp_add(float v) {
  return v + __int_as_float(__builtin_amdgcn_update_dpp(0, __float_as_int(v), CTRL, 0xf, 0xf, true));
}
__device__ __forceinline__ float wave_sum(float v) {
  v = dpp_add<0xB1>(v); v = dpp_add<0x4E>(v); v = dpp_add<0x141>(v); v = dpp_add<0x140>(v);
  auto a = __builtin_amdgcn_permlane16_swap(__float_as_uint(v), __float_as_uint(v), false, false);
  v = __uint_as_float(a[0]) + __uint_as_float(a[1]);
  auto b = __builtin_amdgcn_permlane32_swap(__float_as_uint(v), __float_as_uint(v), false, false);
  return __uint_as_float(b[0]) + __uint_as_float(b[1]);
}

constexpr int SLOT = 16384 + 18432;
constexpr int LDS_TOTAL = 147456;
constexpr int LN_Y = 3 * SLOT, LN_M = LN_Y + 9216, LN_X = LN_M + 9216, LN_H = LN_X + 9216;

struct G1W { unsigned vW0, vW1, vW8; u32x4 rG, rU, rX; };
__device__ __forceinline__ G1W g1w_setup(const u16* wg, const u16* wu, int nb, int tg, int lane) {
  G1W o;
  auto woff = [&](int j) { int pos = 64 * j + lane; int r = pos / 18, c = pos - r * 18; return (unsigned)(pi_row(r) * (F * 2) + nb * 288 + c * 16); };
  o.vW0 = woff(tg); o.vW1 = woff(tg + 4); o.vW8 = woff(8);
  o.rG = make_rsrc(wg, D * F * 2); o.rU = make_rsrc(wu, D * F * 2); o.rX = tg == 1 ? o.rU : o.rG;
  return o;
}
__device__ __forceinline__ unsigned g1w_dst(unsigned ldsbase, int s, int tg, int i) {
  const unsigned dg = ldsbase + (unsigned)(s & 3) * SLOT + 16384, du = dg + 9216;
  return i == 0 ? dg + tg * 1024 : i == 1 ? dg + (tg + 4) * 1024 : i == 2 ? du + tg * 1024 : i == 3 ? du + (tg + 4) * 1024 : (tg == 0 ? dg : du) + 8 * 1024;
}
__device__ __forceinline__ void g1w_ld(const G1W& o, unsigned so, int tg, int i) {
  if (i == 0) DMA(o.vW0, o.rG, so, 0);
  if (i == 1) DMA(o.vW1, o.rG, so, 0);
  if (i == 2) DMA(o.vW0, o.rU, so, 0);
  if (i == 3) DMA(o.vW1, o.rU, so, 0);
  if (i == 4) { if (tg < 2) DMA(o.vW8, o.rX, so, 0); }
}

__device__ __forceinline__ void row_stats(const float (&xv)[10], int l, float& mean, float& rstd) {
  const float p = __int_as_float(__builtin_amdgcn_readfirstlane(__float_as_int(xv[0])));
  float s = 0.f, q = 0.f;
#pragma unroll
  for (int i = 0; i < 10; ++i) { const float d = (i < 8 || l < 32) ? xv[i] - p : 0.f; s += d; q = fmaf(d, d, q); }
  s = wave_sum(s) * (1.f / D); q = wave_sum(q) * (1.f / D);
  mean = rbf(p + s);
  const float var = rbf(fmaxf(q - s * s, 0.f));
  rstd = __builtin_amdgcn_rcpf(rbf(__builtin_amdgcn_sqrtf(rbf(var + 1e-5f))));
}
__device__ __forceinline__ unsigned ln_apply(float x0, float x1, float mean, float rstd, unsigned c1, unsigned sh) {
  f32x2 t = rb2((f32x2){x0, x1} - mean);
  t = rb2(t * rstd);
  t = rb2(t * up2(c1)) + up2(sh);
  return pk2(t.x, t.y);
}

__device__ __forceinline__ void phase_ln(LAS char* lds, const unsigned (&qv)[5], const char* yT, const char* modp, char* x1, char* h2t,
                                         const char* fB, unsigned epoch, int u, int w, int l) {
  const unsigned base = (unsigned)(size_t)lds;
  const unsigned zero = 0;
  if (w == 0) poll_flags<144>(fB, epoch, l);
  else if (w == 1) {
    poll_flags<54>(fB + 144 * 4, epoch, l);
    const u32x4 rM = make_rsrc(modp, 9216);
    const unsigned vo = (unsigned)(l * 16);
    SETM0(base + LN_M); NOP0(); DMAC(vo, rM, zero, 0); DMAC(vo, rM, zero, 1024); DMAC(vo, rM, zero, 2048); DMAC(vo, rM, zero, 3072);
    { const unsigned so = 4096; SETM0(base + LN_M + 4096); NOP0(); DMAC(vo, rM, so, 0); DMAC(vo, rM, so, 1024); DMAC(vo, rM, so, 2048); DMAC(vo, rM, so, 3072); }
    { const unsigned so = 8192; SETM0(base + LN_M + 8192); NOP0(); DMAC(vo, rM, so, 0); }
  }
  float xv[10];
#pragma unroll
  for (int i = 0; i < 5; ++i) { xv[2 * i] = lo_f(qv[i]); xv[2 * i + 1] = hi_f(qv[i]); }
  float mean, rstd;
  row_stats(xv, l, mean, rstd);
  asm volatile("" : "+v"(mean), "+v"(rstd));

  BAR();

  {
    const u32x4 rY = make_rsrc(yT, D * S * 2);
    { const int P = 64 * w + l, d = P < 288 ? 2 * P : 2 * (P - 288) + 1; const unsigned vo = (unsigned)(d * 4096 + u * 16); SETM0(base + LN_Y + w * 1024); NOP0(); DMAC(vo, rY, zero, 0); }
    if (w == 0) { const int P = 512 + l, d = 2 * (P - 288) + 1; const unsigned vo = (unsigned)(d * 4096 + u * 16); SETM0(base + LN_Y + 8192); NOP0(); DMAC(vo, rY, zero, 0); }
  }
  if (w == 1) WAIT_V(1);
  BAR();
  unsigned hp[5], ga[5], sb[5], cb[5];
#pragma unroll
  for (int i = 0; i < 5; ++i) {
    const int c = (i < 4 || l < 32) ? l + 64 * i : 0;
    const u32x4 mA = *(LAS const u32x4*)(lds + LN_M + c * 32);
    cb[i] = *(LAS const unsigned*)(lds + LN_M + c * 32 + 16);
    hp[i] = ln_apply(xv[2 * i], xv[2 * i + 1], mean, rstd, mA.y, mA.x);
    ga[i] = mA.z; sb[i] = mA.w;
    asm volatile("" : "+v"(hp[i]), "+v"(ga[i]), "+v"(sb[i]), "+v"(cb[i]));
  }
  WAIT_V(0); BAR();

#pragma unroll
  for (int i = 0; i < 5; ++i) {
    const bool on = (i < 4 || l < 32);
    const int c = on ? l + 64 * i : 0;
    const float y0 = bf2f(*(LAS const u16*)(lds + LN_Y + c * 16 + w * 2)), y1 = bf2f(*(LAS const u16*)(lds + LN_Y + (288 + c) * 16 + w * 2));
    f32x2 t = rb2(up2(hp[i]) * (f32x2){y0, y1});
    t = rb2(up2(ga[i]) * t) + (f32x2){xv[2 * i], xv[2 * i + 1]};
    const unsigned xp = pk2(t.x, t.y);
    if (on) *(LAS unsigned*)(lds + LN_X + w * 1152 + c * 4) = xp;
    xv[2 * i] = on ? lo_f(xp) : 0.f; xv[2 * i + 1] = on ? hi_f(xp) : 0.f;
  }
  row_stats(xv, l, mean, rstd);
#pragma unroll
  for (int i = 0; i < 5; ++i) {
    const int c = l + 64 * i;
    const unsigned hh = ln_apply(xv[2 * i], xv[2 * i + 1], mean, rstd, cb[i], sb[i]);
    if (i < 4 || l < 32) *(LAS unsigned*)(lds + LN_H + (c >> 3) * 256 + w * 32 + (c & 7) * 4) = hh;
  }
  WAIT_L0(); BAR();

  const int t = w * 64 + l;
  {
    st16_dev(x1 + (size_t)u * 9216 + t * 16, *(LAS const u32x4*)(lds + LN_X + t * 16));
    st16_dev(h2t + ((size_t)(t >> 4) * S + u * 8) * 32 + (t & 15) * 16, *(LAS const u32x4*)(lds + LN_H + t * 16));
    if (t < 64) { const int t2 = 512 + t; st16_dev(x1 + (size_t)u * 9216 + t2 * 16, *(LAS const u32x4*)(lds + LN_X + t2 * 16)); }
    else if (t < 128) { const int t2 = 448 + t; st16_dev(h2t + ((size_t)(t2 >> 4) * S + u * 8) * 32 + (t2 & 15) * 16, *(LAS const u32x4*)(lds + LN_H + t2 * 16)); }
  }
}

template <int NT, int GRP>
__device__ __forceinline__ void g1_body(LAS char* lds, const char* h2t, const u16* wg, const u16* wu, char* act, char* ws,
                                        const char* fL, const char* fP, unsigned epoch, int tb, int nb, int tg, int lane) {
  const int n = lane & 15, g = lane >> 4, q = (lane & 15) >> 2, p = lane & 3;
  constexpr int TAU0 = GRP == 0 ? 0 : 5;
  const unsigned ldsbase = (unsigned)(size_t)lds;
  const u32x4 rH = make_rsrc(h2t, S * D * 2);
  unsigned vH = 0;
  G1W W;
  if (GRP == 0) vH = (unsigned)(((tg >> 1) * S + tb * 256 + (tg & 1) * 128) * 32 + lane * 16);
  else W = g1w_setup(wg, wu, nb, tg, lane);
  constexpr int NDMA = GRP == 0 ? 4 : 5;
  auto dma_m0 = [&](int s, int i) {
    unsigned dst;
    if (GRP == 0) dst = ldsbase + (unsigned)(s & 3) * SLOT + (unsigned)(tg >> 1) * 8192 + (unsigned)(tg & 1) * 4096;
    else dst = g1w_dst(ldsbase, s, tg, i);
    SETM0(dst);
  };
  auto dma_ld = [&](int s, int i) {
    if (GRP == 0) {
      const unsigned so = (unsigned)s * (2 * S * 32);
      if (i == 0) DMAC(vH, rH, so, 0);
      if (i == 1) DMAC(vH, rH, so, 1024);
      if (i == 2) DMAC(vH, rH, so, 2048);
      if (i == 3) DMAC(vH, rH, so, 3072);
    } else g1w_ld(W, (unsigned)s * (32 * F * 2), tg, i);
  };
  if (GRP == 0 && tg == 1) poll_flags<112>(fP, epoch, lane);
  wait_flags<32>(fL, epoch, GRP * 4 + tg, lane);

  if (GRP == 0) {
#pragma unroll
    for (int s = 0; s < 3; ++s)
#pragma unroll
      for (int i = 0; i < 4; ++i) { dma_m0(s, i); NOP0(); dma_ld(s, i); }
  }
  const unsigned aH = (unsigned)((g >> 1) * 8192 + (tg * 64 + n) * 32 + (g & 1) * 16);
  const unsigned aW0 = (unsigned)(16384 + pi_row(8 * g + q) * 288 + TAU0 * 32 + p * 8);
  const unsigned aW1 = (unsigned)(16384 + pi_row(8 * g + 4 + q) * 288 + TAU0 * 32 + p * 8);
  f32x4 acc[4][2 * NT];
#pragma unroll
  for (int i = 0; i < 4; ++i)
#pragma unroll
    for (int j = 0; j < 2 * NT; ++j) acc[i][j] = (f32x4){0.f, 0.f, 0.f, 0.f};
  bf16x8 hfa[4], hfb[4];
  s16x4 wlo[2 * NT], whi[2 * NT];
  unsigned cH = ldsbase + aH, cW0 = ldsbase + aW0, cW1 = ldsbase + aW1;
  auto rdH = [&](bf16x8 (&h)[4], int i) { h[i] = lds_b128(cH + i * 512); };
  auto woffs = [&](int j) { return (j < NT ? j * 32 : 9216 + (j - NT) * 32); };
  auto rdWlo = [&](int j) { wlo[j] = lds_tr(cW0 + woffs(j)); };
  auto rdWhi = [&](int j) { whi[j] = lds_tr(cW1 + woffs(j)); };
#define WAITN(a4, a5) do { if (GRP == 0) WAIT_V(a4); else { if (tg < 2) WAIT_V(a5); else WAIT_V(a4); } } while (0)
  if (GRP == 0) WAIT_V(8); else WAIT_V(0);
  BAR();

#pragma unroll
  for (int i = 0; i < 4; ++i) rdH(hfa, i);
#pragma unroll
  for (int j = 0; j < 2 * NT - 1; ++j) { rdWlo(j); rdWhi(j); }
  WAIT_L0();
  cH += SLOT;
  auto step = [&](int s, bf16x8 (&hc)[4], bf16x8 (&hn)[4]) {
    WAITN(4, 5);
    BAR();
    if (GRP == 1) {
#pragma unroll
      for (int i = 0; i < 5; ++i) { dma_m0(s + 3, i); NOP0(); dma_ld(s + 3, i); }
      SB();
    }
    const int dnext = (((s + 1) & 3) == 0) ? -3 * SLOT : SLOT;
    const int dnext2 = (((s + 2) & 3) == 0) ? -3 * SLOT : SLOT;
#pragma unroll
    for (int j = 0; j < 2 * NT; ++j) {
      const int jr = j == 0 ? 2 * NT - 1 : j - 1;
      const bf16x8 w = cat8(wlo[j], whi[j]);
      MFMA_ASM(acc[0][j], w, hc[0]);
      SB();
      rdWlo(jr);
      SB();
      MFMA_ASM(acc[1][j], w, hc[1]);
      SB();
      rdWhi(jr);
      if (j == 0) { cW0 += dnext; cW1 += dnext; asm volatile("" : "+v"(cW0), "+v"(cW1)); }
      SB();
      MFMA_ASM(acc[2][j], w, hc[2]);
      SB();
      if (j >= 1 && j <= 4) rdH(hn, j - 1);
      if (j == 5) { cH += dnext2; asm volatile("" : "+v"(cH)); }
      SB();
      MFMA_ASM(acc[3][j], w, hc[3]);
      SB();
    }
    if (GRP == 0) {
      dma_m0(s + 3, 0); NOP0();
#pragma unroll
      for (int i = 0; i < 4; ++i) dma_ld(s + 3, i);
      SB();
    }
  };
  for (int s = 0; s < 18; s += 2) { step(s, hfa, hfb); step(s + 1, hfb, hfa); }
  asm volatile("s_nop 7\n\ts_nop 7" ::: "memory");
  SB();

  const size_t tok0 = (size_t)tb * 256 + tg * 64;
#pragma unroll
  for (int j = 0; j < NT; ++j) {
    const int fb = nb * 9 + TAU0 + j;
#pragma unroll
    for (int ip = 0; ip < 2; ++ip) {
      unsigned pkd[2][2];
#pragma unroll
      for (int h = 0; h < 2; ++h) {
        const f32x4 gg = acc[2 * ip + h][j], uu = acc[2 * ip + h][NT + j];
        float a[4];
#pragma unroll
        for (int r = 0; r < 4; ++r) {
          const float e = __builtin_amdgcn_exp2f(gg[r] * -1.44269504089f);
          a[r] = uu[r] * __builtin_amdgcn_rcpf(1.f + e);
        }
        pkd[h][0] = pk2(a[0], a[1]); pkd[h][1] = pk2(a[2], a[3]);
      }
      auto r0 = __builtin_amdgcn_permlane16_swap(pkd[0][0], pkd[1][0], false, false);
      auto r1 = __builtin_amdgcn_permlane16_swap(pkd[0][1], pkd[1][1], false, false);
      u32x4 o; o.x = r0[0]; o.y = r1[0]; o.z = r0[1]; o.w = r1[1];
      const size_t tok = tok0 + (2 * ip + (g & 1)) * 16 + n;
      st16_dev(act + ((size_t)fb * S + tok) * 32 + (g >> 1) * 16, o);
    }
  }
}

template <int NT>
__device__ __forceinline__ void g2_body(LAS char* lds, const char* act, const char* wdt, const char* x1, const char* modp, u16* out, char* ws,
                                        const char* fG, unsigned epoch, int tok0, int ct, int kq, int w, int lane) {
  const int n = lane & 15, g = lane >> 4;
  constexpr int GRP = NT == 5 ? 0 : 1;
  constexpr int TAU0 = NT == 5 ? 0 : 5;
  const unsigned ldsbase = (unsigned)(size_t)lds;
  const u32x4 rA = make_rsrc(act, S * F * 2);
  const u32x4 rW = make_rsrc(wdt, F * D * 2);
  unsigned vA = 0, vW = 0;
  if (GRP == 0) vA = (unsigned)((w * S + tok0) * 32 + lane * 16);
  else vW = (unsigned)((w < 2 ? w * 5 : 10 + (w - 2) * 4) * 1024 + lane * 16);
  unsigned soA0 = __builtin_amdgcn_readfirstlane((unsigned)kq * (72 * S * 32)), soW0 = __builtin_amdgcn_readfirstlane((unsigned)(ct * 144 + kq * 36) * 9216);
  asm volatile("s_nop 4" : "+s"(soA0), "+s"(soW0));
  auto dma = [&](int s) {
    const unsigned slot = ldsbase + (unsigned)(s & 3) * SLOT;
    const unsigned oob = s < 18 ? 0u : 0x40000000u;
    if (GRP == 0) {
      const unsigned so = soA0 + (unsigned)s * (4 * S * 32) + oob;
      SETM0(slot + (unsigned)w * 4096); NOP0();
      DMAC(vA, rA, so, 0); DMAC(vA, rA, so, 1024); DMAC(vA, rA, so, 2048); DMAC(vA, rA, so, 3072);
    } else {
      const unsigned so = soW0 + (unsigned)s * 18432 + oob;
      const unsigned dst = slot + 16384 + (unsigned)(w < 2 ? w * 5 : 10 + (w - 2) * 4) * 1024;
      SETM0(dst); NOP0();
      DMAC(vW, rW, so, 0); DMAC(vW, rW, so, 1024); DMAC(vW, rW, so, 2048); DMAC(vW, rW, so, 3072);
      if (w < 2) { const unsigned so4 = so + 4096; SETM0(dst + 4096); NOP0(); DMAC(vW, rW, so4, 0); }
    }
  };
  if (GRP == 1) { dma(0); dma(1); dma(2); dma(3); }

  wait_flags<8>(fG, epoch, GRP * 4 + w, lane);

  if (GRP == 0) { dma(0); dma(1); dma(2); dma(3); }
  const unsigned aH = (unsigned)((g >> 1) * 4096 + (w * 32 + n) * 32 + (g & 1) * 16);
  const unsigned aW0 = (unsigned)(16384 + TAU0 * 1024 + lane * 16);
  f32x4 acc[2][NT];
#pragma unroll
  for (int i = 0; i < 2; ++i)
#pragma unroll
    for (int j = 0; j < NT; ++j) acc[i][j] = (f32x4){0.f, 0.f, 0.f, 0.f};
  struct Frags { bf16x8 h[2][2]; bf16x8 w[2][NT]; };
  Frags fa, fb;
  unsigned cH = ldsbase + aH, cW0 = ldsbase + aW0;
  auto rdH = [&](Frags& f, int kh, int i) { f.h[kh][i] = lds_b128(cH + kh * 8192 + i * 512); };
  auto rdW = [&](Frags& f, int kh, int j) { f.w[kh][j] = lds_b128(cW0 + kh * 9216 + j * 1024); };
#define WAITN2(a4, a5) do { if (GRP == 0) WAIT_V(a4); else { if (w < 2) WAIT_V(a5); else WAIT_V(a4); } } while (0)
  WAITN2(12, 15);
  BAR();

#pragma unroll
  for (int kh = 0; kh < 2; ++kh) {
    rdH(fa, kh, 0); rdH(fa, kh, 1);
#pragma unroll
    for (int j = 0; j < NT; ++j) rdW(fa, kh, j);
  }
  cH += SLOT; cW0 += SLOT;
  auto step = [&](int s, Frags& fc, Frags& fn) {
    WAIT_L0();
    WAITN2(8, 10);
    BAR();
    const int dnext2 = (((s + 2) & 3) == 0) ? -3 * SLOT : SLOT;
    if (GRP == 1) { dma(s + 4); SB(); }
    auto rd1 = [&](int r_) { constexpr int PER = 2 + NT; const int kh = r_ / PER, r = r_ % PER; if (r < 2) rdH(fn, kh, r); else rdW(fn, kh, r - 2); };
    auto filler = [&](int k) {
      constexpr int PER = 2 + NT;
      if (GRP == 0) { if (k < 2 * PER) rd1(k); }
      else { if (2 * k < 2 * PER) { rd1(2 * k); rd1(2 * k + 1); } }
      if (k == 4 * NT - 1) { cH += dnext2; cW0 += dnext2; asm volatile("" : "+v"(cH), "+v"(cW0)); }
    };
#pragma unroll
    for (int kh = 0; kh < 2; ++kh)
#pragma unroll
      for (int j = 0; j < NT; ++j)
#pragma unroll
        for (int i = 0; i < 2; ++i) { MFMA_ASM(acc[i][j], fc.w[kh][j], fc.h[kh][i]); SB(); filler((kh * NT + j) * 2 + i); SB(); }
    if (GRP == 0) { dma(s + 4); SB(); }
  };
  if (GRP == 0) __builtin_amdgcn_s_setprio(1);
  for (int s = 0; s < 18; s += 2) { step(s, fa, fb); step(s + 1, fb, fa); }
  __builtin_amdgcn_s_setprio(0);
  asm volatile("s_nop 7\n\ts_nop 7" ::: "memory");
  SB();

  const int T = (tok0 >> 7) * 4 + ct;
  constexpr int SLAB = 9216, POFF = NT == 5 ? 0 : 5120;
  char* part = ws + WS_PART;
  if (w != kq) {
    char* dst = part + ((size_t)(T * 4 + w) * 4 + kq) * SLAB + POFF + lane * 16;
#pragma unroll
    for (int j = 0; j < NT; ++j) {
      u32x4 o; o.x = pk2(acc[0][j][0], acc[0][j][1]); o.y = pk2(acc[0][j][2], acc[0][j][3]); o.z = pk2(acc[1][j][0], acc[1][j][1]); o.w = pk2(acc[1][j][2], acc[1][j][3]);
      st16_dev(dst + j * 1024, o);
    }
  }

  WAIT_V(0); BAR();
  char* fX = ws + WS_FX;
  if (threadIdx.x < 4 && (int)threadIdx.x != kq) st4_dev(fX + ((size_t)(T * 4 + threadIdx.x) * 16 + kq) * 4, epoch);

  if (w != kq) {
    if (blockIdx.x == 0 && GRP == 0 && w == (kq == 0 ? 1 : 0)) {
      poll_flags<256>(ws + WS_FL, epoch, lane);
      if (lane == 0) st4_dev(ws + WS_EPOCH, epoch);
    }
    return;
  }
  const int tokb = tok0 + 32 * kq;
  u32x2 xr[2][NT]; unsigned gb[NT][2];
  {
#pragma unroll
    for (int j = 0; j < NT; ++j) {
      const int col = ct * 144 + (TAU0 + j) * 16 + 4 * g;
#pragma unroll
      for (int i = 0; i < 2; ++i) asm volatile("global_load_dwordx2 %0, %1, off sc1" : "=v"(xr[i][j]) : "v"(x1 + ((size_t)(tokb + 16 * i + n) * D + col) * 2) : "memory");
      asm volatile("global_load_dword %0, %1, off sc1" : "=v"(gb[j][0]) : "v"(modp + (col >> 1) * 32 + 20) : "memory");
      asm volatile("global_load_dword %0, %1, off sc1" : "=v"(gb[j][1]) : "v"(modp + ((col >> 1) + 1) * 32 + 20) : "memory");
    }
  }
  poll_flags<4>(fX + (size_t)(T * 4 + kq) * 64, epoch, lane, kq);

  f32x4 pp[3][2][NT];
  u32x4 pb[3][NT];
#pragma unroll
  for (int sidx = 0; sidx < 3; ++sidx) {
    const int src = sidx + (sidx >= kq ? 1 : 0);
    const char* sp = part + ((size_t)(T * 4 + kq) * 4 + src) * SLAB + POFF + lane * 16;
#pragma unroll
    for (int j = 0; j < NT; ++j) asm volatile("global_load_dwordx4 %0, %1, off sc1" : "=v"(pb[sidx][j]) : "v"(sp + j * 1024) : "memory");
  }
  WAIT_V(0);
  SB();

#pragma unroll
  for (int sidx = 0; sidx < 3; ++sidx)
#pragma unroll
    for (int j = 0; j < NT; ++j) {
      asm volatile("" : "+v"(pb[sidx][j]));
      pp[sidx][0][j] = (f32x4){lo_f(pb[sidx][j].x), hi_f(pb[sidx][j].x), lo_f(pb[sidx][j].y), hi_f(pb[sidx][j].y)};
      pp[sidx][1][j] = (f32x4){lo_f(pb[sidx][j].z), hi_f(pb[sidx][j].z), lo_f(pb[sidx][j].w), hi_f(pb[sidx][j].w)};
    }
#pragma unroll
  for (int j = 0; j < NT; ++j) {
    asm volatile("" : "+v"(gb[j][0]), "+v"(gb[j][1]), "+v"(xr[0][j]), "+v"(xr[1][j]));
    unsigned pk[2][2];
#pragma unroll
    for (int i = 0; i < 2; ++i) {
      const f32x4 sum = ((acc[i][j] + pp[0][i][j]) + pp[1][i][j]) + pp[2][i][j];
      const unsigned d01 = pk2(sum[0], sum[1]), d23 = pk2(sum[2], sum[3]);
      const unsigned m01 = pk2(lo_f(gb[j][0]) * lo_f(d01), hi_f(gb[j][0]) * hi_f(d01)), m23 = pk2(lo_f(gb[j][1]) * lo_f(d23), hi_f(gb[j][1]) * hi_f(d23));
      pk[i][0] = pk2(lo_f(xr[i][j].x) + lo_f(m01), hi_f(xr[i][j].x) + hi_f(m01));
      pk[i][1] = pk2(lo_f(xr[i][j].y) + lo_f(m23), hi_f(xr[i][j].y) + hi_f(m23));
    }
    auto r0 = __builtin_amdgcn_permlane16_swap(pk[0][0], pk[1][0], false, false);
    auto r1 = __builtin_amdgcn_permlane16_swap(pk[0][1], pk[1][1], false, false);
    u32x4 o; o.x = r0[0]; o.y = r1[0]; o.z = r0[1]; o.w = r1[1];
    const int tok = tokb + 16 * (g & 1) + n, col = ct * 144 + (TAU0 + j) * 16 + 8 * (g >> 1);
    *(u32x4*)(out + (size_t)tok * D + col) = o;
  }
}

__global__ __launch_bounds__(512, 2) void k_mega(const u16* q, const u16* k, const u16* v, const u16* sk, const u16* fk, const u16* Dv,
                                                 const u16* wg, const u16* wu, const u16* wd, const u16* c, const u16* wm,
                                                 u16* out, char* ws) {
  extern __shared__ __attribute__((aligned(16))) char shm[];
  LAS char* lds = (LAS char*)shm;

  const int wid = __builtin_amdgcn_readfirstlane(threadIdx.x >> 6);
  const int b = blockIdx.x, x = b & 7, i = b >> 3;
  unsigned cpf = 0;
  if (wid == 7 && i >= 24) {
    unsigned long long pc; unsigned len;
    asm volatile("s_getpc_b64 %0\nk_mega_code_begin:\n\ts_mov_b32 %1, k_mega_code_end-k_mega_code_begin" : "=s"(pc), "=s"(len));
    unsigned off = (unsigned)((i - 24) * 64 + (threadIdx.x & 63)) * 128u;
    off = off < len ? off : 0u;
    asm volatile("global_load_dword %0, %1, off\n\ts_waitcnt vmcnt(0)" : "=&v"(cpf) : "v"((const char*)pc + off) : "memory");
  }
  asm volatile("" :: "s"(q), "s"(k), "s"(v), "s"(sk), "s"(fk), "s"(Dv), "s"(wg), "s"(wu), "s"(wd), "s"(c), "s"(wm), "s"(out), "s"(ws));
  const unsigned epoch_prev = *(const __attribute__((address_space(4))) unsigned*)(size_t)(ws + WS_EPOCH);
  unsigned epoch;
  auto get_epoch = [&]() { asm volatile("s_add_u32 %0, %1, 1" : "=s"(epoch) : "s"(epoch_prev) : "scc"); };
  unsigned qv[5];
  auto load_q = [&]() {
    const int l = opaque_tid() & 63;
#pragma unroll
    for (int j = 0; j < 5; ++j) qv[j] = (j < 4 || l < 32) ? *(const unsigned*)(q + (size_t)((x * 32 + i) * 8 + wid) * D + 2 * (l + 64 * j)) : 0u;
  };
  if (b < 144) {
    const int lane = opaque_tid() & 63;
    phase_kv(lds + CV_STAGE, k, v, sk, fk, ws + WS_KVT, b >> 4, b & 15, 4 * b, wid, lane);
    get_epoch();
    publish(ws + WS_FA + ((b >> 4) * 32 + (b & 15)) * 4, 1, 0, epoch);

    conv_load_f(lds, fk, 4 * b, wid, lane);
    conv_front(lds, fk, 4 * b, wid, lane);
    wait_flags<16>(ws + WS_FA + (b >> 4) * 128, epoch, wid, lane);

    phase_conv(lds, ws + WS_KVT, Dv, ws + WS_YT, 4 * b, wid, lane, load_q);
    publish(ws + WS_FB + b * 4, 16, 1024, epoch);
  } else {
    const int lane = opaque_tid() & 63;
    int unit = -1;
    get_epoch();
    {
      const unsigned long long t0_ = __builtin_amdgcn_s_memrealtime();
      while (__builtin_amdgcn_s_memrealtime() - t0_ < (unsigned long long)(b < 198 ? 300 : 400)) __builtin_amdgcn_s_sleep(8);
    }
    if (b < 198) {
      phase_mod(lds, c, wm, ws + WS_MOD, b - 144);
      publish(ws + WS_FB + b * 4, 16, 1024, epoch);

      const int j = b - 144;
      if (wid < 2) unit = 464 + 2 * j + wid; else if (wid == 2 && j < 4) unit = 572 + j;
    } else unit = (b - 198) * 8 + wid;
    load_q();
    if (unit >= 0) phase_pretile(lds, wd, ws + WS_WDT, unit, wid, lane);
    publish(ws + WS_FP + b * 4, 16, 1024, epoch);
  }

  const int tb = (x & 1) * 4 + (i & 3), nb = (x >> 1) * 8 + (i >> 2);
  {
    const int lane = opaque_tid() & 63;
    const int uo = x * 32 + i;
#pragma unroll
    for (int j = 0; j < 5; ++j) asm volatile("" : "+v"(qv[j]));
    if (wid >= 4) {
      const G1W W = g1w_setup(wg, wu, nb, wid - 4, lane);
#pragma unroll
      for (int s = 0; s < 3; ++s)
#pragma unroll
        for (int j = 0; j < 5; ++j) { SETM0(g1w_dst((unsigned)(size_t)lds, s, wid - 4, j)); NOP0(); g1w_ld(W, (unsigned)s * (32 * F * 2), wid - 4, j); }
    }
    phase_ln(lds, qv, ws + WS_YT, ws + WS_MOD, ws + WS_X1, ws + WS_H2, ws + WS_FB + (b & 15) * 1024, epoch, uo, wid, lane);
    publish(ws + WS_FL + uo * 4, 4, 1024, epoch);
  }
  {
    const int lane = opaque_tid() & 63;
    const char* fL = ws + WS_FL + (nb & 3) * 1024 + tb * 128;
    const char* fP = ws + WS_FP + (b & 15) * 1024 + 144 * 4;
    if (wid < 4) g1_body<5, 0>(lds, ws + WS_H2, wg, wu, ws + WS_ACT, ws, fL, fP, epoch, tb, nb, wid, lane);
    else g1_body<4, 1>(lds, ws + WS_H2, wg, wu, ws + WS_ACT, ws, fL, fP, epoch, tb, nb, wid - 4, lane);
    publish(ws + WS_FG + (tb * 32 + nb) * 4, 4, 1024, epoch);
  }
  {
    const int lane = opaque_tid() & 63;
    const int th = x & 1, kq = x >> 1;
    const int tok0 = th * 1024 + (i >> 2) * 128, ct = i & 3;
    const char* fG = ws + WS_FG + ct * 1024 + ((tok0 >> 8) * 32 + kq * 8) * 4;
    if (wid < 4) g2_body<5>(lds, ws + WS_ACT, ws + WS_WDT, ws + WS_X1, ws + WS_MOD, out, ws, fG, epoch, tok0, ct, kq, wid, lane);
    else g2_body<4>(lds, ws + WS_ACT, ws + WS_WDT, ws + WS_X1, ws + WS_MOD, out, ws, fG, epoch, tok0, ct, kq, wid - 4, lane);
  }
  asm volatile("k_mega_code_end:");
}

extern "C" void kernel_launch(void* const* d_in, const int* in_sizes, int n_in,
                              void* d_out, int out_size, void* d_ws, size_t ws_size,
                              hipStream_t stream) {
  (void)hipFuncSetAttribute((const void*)k_mega, hipFuncAttributeMaxDynamicSharedMemorySize, LDS_TOTAL);
  k_mega<<<256, 512, LDS_TOTAL, stream>>>((const u16*)d_in[0], (const u16*)d_in[1], (const u16*)d_in[2], (const u16*)d_in[3],
                                          (const u16*)d_in[4], (const u16*)d_in[5], (const u16*)d_in[7], (const u16*)d_in[8],
                                          (const u16*)d_in[9], (const u16*)d_in[10], (const u16*)d_in[11], (u16*)d_out, (char*)d_ws);
}
```
